```python
import torch

def _softmax(x, axis=-1):
    return torch.softmax(x, dim=axis)
def _gelu(x):
    return torch.nn.functional.gelu(x, approximate="tanh")
def _silu(x):
    return torch.nn.functional.silu(x)
def _sigmoid(x):
    return torch.sigmoid(x)
def _layernorm(x, axis=-1, eps=1e-5):
    m = x.mean(dim=axis, keepdim=True)
    v = x.var(dim=axis, keepdim=True, unbiased=False)
    return (x - m) / torch.sqrt(v + eps)
def _scatter_add(tgt, idx, src, axis=0):
    return tgt.clone().index_add_(axis, idx, src)
def _segment_sum(src, seg, n):
    out = torch.zeros((n,) + src.shape[1:], dtype=src.dtype)
    return out.index_add_(0, seg, src)
def _pad(x, pw):
    flat = tuple(v for pair in reversed(pw) for v in pair)
    return torch.nn.functional.pad(x, flat)

kernel_name = 'recurrent_scan__sliding_block_cached__dense_activation_squared_relu__decode'

def setup_inputs():

    _w = (lambda a: (lambda t: t.to(torch.bfloat16) if t.is_floating_point() else t)(torch.as_tensor(a)))
    def _A_setup():
        B, Sq, D = 2, 1, 576
        Hq, Hkv, Sk, Dh = 32, 16, 16384, 128
        assert Hq % Hkv == 0, (Hq, Hkv)
        return {
            "x": _w((lambda *s: torch.randn(*s, dtype=torch.bfloat16) * 0.05)(B, Sq, D)),
            "k_cache": _w((lambda *s: torch.randn(*s, dtype=torch.bfloat16) * 0.8)(B, Hkv, Sk, Dh)),
            "v_cache": _w((lambda *s: torch.randn(*s, dtype=torch.bfloat16) * 0.2)(B, Hkv, Sk, Dh)),
            "wq": _w((lambda *s: torch.randn(*s, dtype=torch.bfloat16) * 0.8)(D, Hq * Dh) * (D ** -0.5)),
            "wo": _w((lambda *s: torch.randn(*s, dtype=torch.bfloat16) * 0.2)(Hq * Dh, D) * ((Hq * Dh) ** -0.5)),
        }
    _A_in = _A_setup()

    def _B_setup():
        B, S, D, F = 2, 1, 576, 4608
        return {
            "x": _w((lambda *s: torch.randn(*s, dtype=torch.bfloat16) * 0.05)(B, S, D)),
            "w1": _w((lambda *s: torch.randn(*s, dtype=torch.bfloat16) * 0.1)(D, F)),
            "b1": _w((lambda *s: torch.randn(*s, dtype=torch.bfloat16) * 0.1)(F)),
            "w2": _w((lambda *s: torch.randn(*s, dtype=torch.bfloat16) * 0.1)(F, D)),
            "b2": _w((lambda *s: torch.randn(*s, dtype=torch.bfloat16) * 0.1)(D)),
        }
    _B_in = _B_setup()

    return {
        "A_x": _A_in["x"],
        "A_k_cache": _A_in["k_cache"],
        "A_v_cache": _A_in["v_cache"],
        "A_wq": _A_in["wq"],
        "A_wo": _A_in["wo"],
        "B_x": _B_in["x"],
        "B_w1": _B_in["w1"],
        "B_b1": _B_in["b1"],
        "B_w2": _B_in["w2"],
        "B_b2": _B_in["b2"],
    }

def reference(A_x, A_k_cache, A_v_cache, A_wq, A_wo, B_x, B_w1, B_b1, B_w2, B_b2):
    def _A_ref(x, k_cache, v_cache, wq, wo):
        B, Sq, D = x.shape
        Hq, Dh = 32, 128
        Hkv, Sk = k_cache.shape[1], k_cache.shape[2]
        W = min(4096, max(256, Sk // 4))
        rep = Hq // Hkv
        q = (lambda x, *d: x.permute(*d))((x @ wq).reshape(B, Sq, Hq, Dh), 0, 2, 1, 3)
        k = (lambda x, n, axis: torch.repeat_interleave(x, n, dim=axis))(k_cache[:, :, Sk - W:, :], rep, axis=1)
        v = (lambda x, n, axis: torch.repeat_interleave(x, n, dim=axis))(v_cache[:, :, Sk - W:, :], rep, axis=1)
        scores = torch.einsum("bhqd,bhkd->bhqk", q, k) * (Dh ** -0.5)
        p = _softmax(scores, axis=-1)
        o = torch.einsum("bhqk,bhkd->bhqd", p, v)
        return (lambda x, *d: x.permute(*d))(o, 0, 2, 1, 3).reshape(B, Sq, Hq * Dh) @ wo

    def _B_ref(x, w1, b1, w2, b2):
        h = torch.einsum("bsd,df->bsf", x, w1) + b1
        zero = torch.zeros_like(h)
        r = torch.where(h > zero, h, zero)
        h = r * r
        out = torch.einsum("bsf,fd->bsd", h, w2) + b2
        return out

    x = A_x
    xn = _layernorm(x, axis=-1)
    s = _A_ref(xn, A_k_cache, A_v_cache, A_wq, A_wo)
    g = _silu(_B_ref(xn, B_w1, B_b1, B_w2, B_b2))
    return x + s * g
```

```cpp
#include <hip/hip_runtime.h>
#include <hip/hip_bf16.h>
#include <stdint.h>

typedef unsigned short u16;
typedef unsigned int u32;
typedef unsigned long long u64;
typedef u32 u32x4 __attribute__((ext_vector_type(4)));
typedef float f32x4 __attribute__((ext_vector_type(4)));
typedef float f32x2 __attribute__((ext_vector_type(2)));
typedef short bf16x8 __attribute__((ext_vector_type(8)));
#define GAS __attribute__((address_space(1)))
typedef GAS u64 gu64;
typedef __attribute__((address_space(3))) u32 lu32;
#define RLX_WG __ATOMIC_RELAXED, __HIP_MEMORY_SCOPE_WORKGROUP
#define RLX_AGENT __ATOMIC_RELAXED, __HIP_MEMORY_SCOPE_AGENT


constexpr int D = 576, SK = 16384, WIN = 4096, F = 4608;
constexpr int WS_EPOCH = 0;
constexpr int WS_QG = 32;
constexpr int WS_HG = WS_QG + 16 * 2 * 4 * 256;
constexpr int WS_AO = WS_HG + 32 * 8 * 288;
constexpr int WS_AML = WS_AO + 16 * 16 * 2 * 128;
constexpr int WS_FG = WS_AML + 16 * 2 * 16 * 16;
constexpr int MLS = 16;
constexpr unsigned SPIN_CAP = 1u << 17;

constexpr int L_QS = 0;
constexpr int L_HB = L_QS + 128;
constexpr int L_OB = L_HB + 160;
constexpr int L_SM = L_OB + 128;
constexpr int L_SL = L_SM + 32;
constexpr int L_CNT = L_SL + 32;
constexpr int L_SCL = L_CNT + 4;
constexpr int L_OMRG = L_SCL + 16;
constexpr int L_WST = L_OMRG + 16 * 256;
constexpr int L_RED = L_WST + 2448 * 4;
constexpr int L_XN = L_RED + 8192;
constexpr int L_END = L_XN + 2 * D;
static_assert((L_HB % 4) == 0 && (L_OB % 4) == 0 && (L_SM % 4) == 0 && (L_SCL % 4) == 0 && (L_OMRG % 4) == 0 && (L_WST % 4) == 0 && (L_RED % 4) == 0, "lds");

__device__ __forceinline__ float bflo(u32 w) { return __uint_as_float(w << 16); }
__device__ __forceinline__ float bfhi(u32 w) { return __uint_as_float(w & 0xffff0000u); }
__device__ __forceinline__ float bf2f(u16 b) { return __uint_as_float((u32)b << 16); }
__device__ __forceinline__ float rbf(float f) { return (float)(__bf16)f; }
__device__ __forceinline__ u16 f2bf(float f) { return __builtin_bit_cast(u16, (__bf16)f); }
__device__ __forceinline__ u32 pkbf(float lo, float hi) { return (u32)f2bf(lo) | ((u32)f2bf(hi) << 16); }

template <int CTRL> __device__ __forceinline__ float dpp(float x) {
  return __builtin_bit_cast(float, __builtin_amdgcn_mov_dpp(__builtin_bit_cast(int, x), CTRL, 0xf, 0xf, true));
}
constexpr int ROR1 = 0x121, ROR2 = 0x122, ROR4 = 0x124, ROR8 = 0x128;
__device__ __forceinline__ float row_sum(float x) {
  x += dpp<ROR8>(x); x += dpp<ROR4>(x); x += dpp<ROR2>(x); x += dpp<ROR1>(x); return x;
}
__device__ __forceinline__ float row_max(float x) {
  x = fmaxf(x, dpp<ROR8>(x)); x = fmaxf(x, dpp<ROR4>(x)); x = fmaxf(x, dpp<ROR2>(x)); x = fmaxf(x, dpp<ROR1>(x)); return x;
}
__device__ __forceinline__ float tr32(float a, float b) {
  auto r = __builtin_amdgcn_permlane32_swap(__float_as_uint(a), __float_as_uint(b), false, false);
  return __uint_as_float(r[0]) + __uint_as_float(r[1]);
}
__device__ __forceinline__ float tr16(float a, float b) {
  auto r = __builtin_amdgcn_permlane16_swap(__float_as_uint(a), __float_as_uint(b), false, false);
  return __uint_as_float(r[0]) + __uint_as_float(r[1]);
}
__device__ __forceinline__ float xrow_sum(float x) { x = tr16(x, x); return tr32(x, x); }
__device__ __forceinline__ float xrow_max(float x) {
  auto s = __builtin_amdgcn_permlane16_swap(__float_as_uint(x), __float_as_uint(x), false, false);
  x = fmaxf(__uint_as_float(s[0]), __uint_as_float(s[1]));
  auto t = __builtin_amdgcn_permlane32_swap(__float_as_uint(x), __float_as_uint(x), false, false);
  return fmaxf(__uint_as_float(t[0]), __uint_as_float(t[1]));
}
__device__ __forceinline__ float wave_sum(float x) { return xrow_sum(row_sum(x)); }

__device__ __forceinline__ f32x4 mfma16(u32x4 a, u32x4 b, f32x4 c) {
  return __builtin_amdgcn_mfma_f32_16x16x32_bf16(__builtin_bit_cast(bf16x8, a), __builtin_bit_cast(bf16x8, b), c, 0, 0, 0);
}
__device__ __forceinline__ u32x4 ldnt(const u16* p) { return __builtin_nontemporal_load((const u32x4*)p); }
typedef short s16x4 __attribute__((ext_vector_type(4)));
typedef __attribute__((address_space(3))) s16x4 ls16x4;
typedef u32 u32x2 __attribute__((ext_vector_type(2)));
__device__ __forceinline__ u32x2 trread(const char* p) {
  return __builtin_bit_cast(u32x2, __builtin_amdgcn_ds_read_tr16_b64_v4i16((ls16x4*)p));
}
#define LDS_FENCE() asm volatile("s_waitcnt lgkmcnt(0)" ::: "memory")
#define BARRIER() do { LDS_FENCE(); __builtin_amdgcn_s_barrier(); asm volatile("" ::: "memory"); } while (0)
#define PIN() __builtin_amdgcn_sched_barrier(0)

__device__ __forceinline__ void gput(gu64* p, u64 tag, float v) {
  __hip_atomic_store(p, tag | (u64)(__float_as_uint(v) >> 7), RLX_AGENT);
}
__device__ __forceinline__ u64 gget(gu64* p) { return __hip_atomic_load(p, RLX_AGENT); }
__device__ __forceinline__ bool gok(u64 g, u64 tag) { return (g ^ tag) < (1ull << 25); }
__device__ __forceinline__ float gval(u64 g) { return __uint_as_float((u32)g << 7); }

template <int NKS, int NROW, int VS>
__device__ __forceinline__ f32x4 gemv_tile(const char* wbase, const char* vbase, int ct, int l) {
  const int g = l >> 4, q = (l & 15) >> 2, p = l & 3, m = l & 15;
  f32x4 acc = {0.f, 0.f, 0.f, 0.f};
#pragma unroll
  for (int ks = 0; ks < NKS; ++ks) {
    int r0 = 32 * ks + 8 * g;
    const u32x4 av = *(const u32x4*)(vbase + (m & 1) * (VS * 2) + r0 * 2);
    const u32x4 a = (m < 2) ? av : u32x4{0u, 0u, 0u, 0u};
    if (32 * ks + 32 > NROW) r0 = (r0 + 8 <= NROW) ? r0 : r0 - 16;
    const char* bp = wbase + (r0 + q) * 144 + (16 * ct + 4 * p) * 2;
    const u32x2 b0 = trread(bp), b1 = trread(bp + 4 * 144);
    acc = mfma16(a, u32x4{b0[0], b0[1], b1[0], b1[1]}, acc);
  }
  return acc;
}

__global__ void __launch_bounds__(1024, 4)
fused_decode_kernel(const u16* __restrict__ x, const u16* __restrict__ kc, const u16* __restrict__ vc,
                    const u16* __restrict__ wq, const u16* __restrict__ wo, const u16* __restrict__ w1,
                    const u16* __restrict__ b1, const u16* __restrict__ w2, const u16* __restrict__ b2,
                    u16* __restrict__ out, u64* ws_) {
  __shared__ __attribute__((aligned(16))) float lds[L_END];
  gu64* ws = (gu64*)ws_;
  const int tid = threadIdx.x, l = tid & 63, w = __builtin_amdgcn_readfirstlane(tid >> 6);
  const int g = l >> 4, cc = l & 15;
  const int bid = blockIdx.x, slot = bid >> 3;
  const int h = (bid & 7) * 2 + (slot >> 4), mi = slot & 15, i = mi >> 3, c = mi & 7;
  const int hq = 2 * h + i;
  const u64 epoch = ws_[WS_EPOCH];
  const u64 tag = (epoch & ~((1ull << 25) - 1)) + (1ull << 25);
  const int hidx = (w - 4) * 64 + l;
  u32x4 WS[6];
  float b1f = 0.f;
  lu32* cnt = (lu32*)&lds[L_CNT];
  if (tid < 3) __hip_atomic_store(cnt + tid, 0u, RLX_WG);
  u32 xa[2][5];
  if (w == 8) {
    const u32* xw = (const u32*)x;
#pragma unroll
    for (int b = 0; b < 2; ++b) {
#pragma unroll
      for (int k = 0; k < 4; ++k) xa[b][k] = xw[b * 288 + l + 64 * k];
      xa[b][4] = xw[b * 288 + 256 + (l & 31)];
    }
  }
  PIN();
  BARRIER();
  if (w == 8) {
#pragma unroll
    for (int k = 0; k < 5; ++k) asm volatile("" : "+v"(xa[0][k]), "+v"(xa[1][k]));
    const u16 b1v = b1[hq * 144 + (hidx < 288 ? (hidx % 144) : 0)];
    const float msk = (l < 32) ? 1.f : 0.f;
#pragma unroll
    for (int b = 0; b < 2; ++b) {
      float s = 0.f;
#pragma unroll
      for (int k = 0; k < 4; ++k) s += bflo(xa[b][k]) + bfhi(xa[b][k]);
      s += msk * (bflo(xa[b][4]) + bfhi(xa[b][4]));
      const float mean = wave_sum(s) * (1.f / D);
      float q = 0.f;
#pragma unroll
      for (int k = 0; k < 4; ++k) { const float a = bflo(xa[b][k]) - mean, e = bfhi(xa[b][k]) - mean; q += a * a + e * e; }
      { const float a = bflo(xa[b][4]) - mean, e = bfhi(xa[b][4]) - mean; q += msk * (a * a + e * e); }
      const float var = wave_sum(q) * (1.f / D);
      const float mb = rbf(mean), rden = 1.f / rbf(sqrtf(rbf(rbf(var) + 1e-5f)));
#pragma unroll
      for (int k = 0; k < 5; ++k) {
        const f32x2 v = {rbf(rbf(bflo(xa[b][k]) - mb) * rden), rbf(rbf(bfhi(xa[b][k]) - mb) * rden)};
        if (k < 4 || l < 32) *(f32x2*)&lds[L_XN + b * D + 2 * (l + 64 * k)] = v;
      }
    }
    LDS_FENCE();
    if (l == 0) __hip_atomic_store(cnt + 2, 1u, RLX_WG);
    b1f = bf2f(b1v);
    asm volatile("" : "+v"(b1f));
  } else if (w < 8) {
    const bool isq = w < 4;
    const int t = tid & 255, rl = isq ? (t >> 3) : (t / 18), ch = isq ? (t & 7) : (t - rl * 18);
    const int row0 = (isq ? (mi & 3) * 144 : c * 72) + rl, rstep = isq ? 32 : 12;
    const int nv = isq ? (rl < 16 ? 5 : 4) : (t < 216 ? 6 : 0);
    const u16* pb = isq ? (wq + (size_t)row0 * 4096 + h * 256 + (mi >> 2) * 64 + ch * 8) : (w1 + (size_t)row0 * F + hq * 144 + ch * 8);
    const int pstep = isq ? 32 * 4096 : 12 * F;
    const u16 b1v = b1[hq * 144 + (isq ? 0 : (hidx % 144))];
    u32x4 PW[6];
#pragma unroll
    for (int k = 0; k < 6; ++k) PW[k] = ldnt(k < nv ? pb + k * pstep : x);
    PIN();
    while (__hip_atomic_load(cnt + 2, RLX_WG) == 0u) __builtin_amdgcn_s_sleep(1);
    asm volatile("" ::: "memory");
    b1f = bf2f(b1v);
    asm volatile("" : "+v"(b1f));
    float a[16];
#pragma unroll
    for (int k = 0; k < 16; ++k) a[k] = 0.f;
#pragma unroll
    for (int k = 0; k < 6; ++k) {
      const int kk = k < nv ? k : 0;
      float x0 = lds[L_XN + row0 + kk * rstep], x1 = lds[L_XN + D + row0 + kk * rstep];
      x0 = k < nv ? x0 : 0.f; x1 = k < nv ? x1 : 0.f;
#pragma unroll
      for (int e = 0; e < 4; ++e) {
        const float e0 = bflo(PW[k][e]), e1 = bfhi(PW[k][e]);
        a[2 * e] = fmaf(x0, e0, a[2 * e]); a[2 * e + 1] = fmaf(x0, e1, a[2 * e + 1]);
        a[8 + 2 * e] = fmaf(x1, e0, a[8 + 2 * e]); a[8 + 2 * e + 1] = fmaf(x1, e1, a[8 + 2 * e + 1]);
      }
    }
    {
      f32x4* r = (f32x4*)&lds[L_RED + tid * 16];
#pragma unroll
      for (int k = 0; k < 4; ++k) r[k] = f32x4{a[4 * k], a[4 * k + 1], a[4 * k + 2], a[4 * k + 3]};
      LDS_FENCE();
      if (l == 0) __hip_atomic_fetch_add(cnt, 1u, RLX_WG);
    }
    if (w < 7) {
      while (__hip_atomic_load(cnt, RLX_WG) < 8u) __builtin_amdgcn_s_sleep(1);
      asm volatile("" ::: "memory");
      const bool pq = w < 2;
      const int o = pq ? tid : (tid - 128), oo = (pq || o < 288) ? o : 0;
      const int b = pq ? (oo >> 6) : (oo / 144), col = pq ? (oo & 63) : (oo - b * 144);
      const float* r = &lds[L_RED + ((pq ? 0 : 256) + (col >> 3)) * 16 + b * 8 + (col & 7)];
      const int rs = pq ? 128 : 288, n = pq ? 32 : 12;
      float s0 = 0.f, s1 = 0.f, s2 = 0.f, s3 = 0.f;
      for (int k = 0; k < n; k += 4) { s0 += r[k * rs]; s1 += r[(k + 1) * rs]; s2 += r[(k + 2) * rs]; s3 += r[(k + 3) * rs]; }
      gu64* dst = ws + (pq ? (WS_QG + ((h * 2 + b) * 4 + (mi & 3)) * 256 + (mi >> 2) * 64 + col) : (WS_HG + (hq * 8 + c) * 288 + oo));
      if (pq || o < 288) gput(dst, tag, (s0 + s1) + (s2 + s3));
    }
  } else {
#pragma unroll
    for (int k = 0; k < 6; ++k) {
      const int id = (tid - 576) + 448 * k;
      const bool isa = id < 1152, isb = !isa && id < 2448;
      const int id2 = isa ? id : (isb ? id - 1152 : 0);
      const int r = id2 / 9, ch = id2 - r * 9;
      const u16* p = isa ? (wo + (size_t)(hq * 128 + r) * D) : (w2 + (size_t)(hq * 144 + r) * D);
      WS[k] = ldnt(p + c * 72 + ch * 8);
    }
  }
  PIN();
  const int wr = (w + bid) & 15;
  const size_t kvoff = ((size_t)(i * 16 + h) * SK + (SK - WIN) + c * 512 + wr * 32) * 128;
  u32x4 K[8], V[8];
  {
    const u16* kp = kc + kvoff + (cc & 7) * 128 + (cc >> 3) * 32 + g * 8;
#pragma unroll
    for (int n = 0; n < 8; ++n) K[n] = ldnt(kp + (n >> 1) * 8 * 128 + (n & 1) * 64);
  }
  gu64* gsrc = ws + ((w < 4) ? (WS_QG + (h * 2 + i) * 1024 + tid) : (WS_HG + hq * 2304 + (hidx < 288 ? hidx : 0)));
  u64 gq[4];
  if (w < 4) {
#pragma unroll
    for (int k = 0; k < 4; ++k) gq[k] = gget(gsrc + k * 256);
  }
  PIN();
  __builtin_amdgcn_s_barrier();
  PIN();
  {
    const u16* vp = vc + kvoff + (4 * g) * 128 + cc * 8;
#pragma unroll
    for (int j = 0; j < 8; ++j) V[j] = ldnt(vp + (16 * (j >> 2) + (j & 3)) * 128);
  }
  PIN();
  if (w < 4) {
    bool ok = gok(gq[0], tag) & gok(gq[1], tag) & gok(gq[2], tag) & gok(gq[3], tag);
    float s = (gval(gq[0]) + gval(gq[1])) + (gval(gq[2]) + gval(gq[3]));
    if (!__all(ok)) {
      for (unsigned spins = 0; spins < SPIN_CAP; ++spins) {
        const u64 t0 = gget(gsrc), t1 = gget(gsrc + 256), t2 = gget(gsrc + 512), t3 = gget(gsrc + 768);
        ok = gok(t0, tag) & gok(t1, tag) & gok(t2, tag) & gok(t3, tag);
        s = (gval(t0) + gval(t1)) + (gval(t2) + gval(t3));
        if (__all(ok)) break;
        __builtin_amdgcn_s_sleep(8);
      }
    }
    ((u16*)&lds[L_QS])[tid] = f2bf(s);
    LDS_FENCE();
    if (l == 0) __hip_atomic_fetch_add(cnt + 1, 1u, RLX_WG);
  } else if (w >= 9) {
    u32x4* wst = (u32x4*)&lds[L_WST] + (tid - 576);
#pragma unroll
    for (int k = 0; k < 5; ++k) wst[448 * k] = WS[k];
    if (tid - 576 + 448 * 5 < 2448) wst[448 * 5] = WS[5];
  }
  while (__hip_atomic_load(cnt + 1, RLX_WG) < 4u) __builtin_amdgcn_s_sleep(1);
  asm volatile("" ::: "memory");
  {
    u32x4 qf[4];
    const u32x4* qs4 = (const u32x4*)&lds[L_QS];
#pragma unroll
    for (int s = 0; s < 4; ++s) {
      const u32x4 t = qs4[(cc & 1) * 16 + s * 4 + g];
      qf[s] = (cc < 2) ? t : u32x4{0u, 0u, 0u, 0u};
    }
    float sc[8];
    float mx = -INFINITY;
#pragma unroll
    for (int t = 0; t < 2; ++t) {
      f32x4 xu[2];
#pragma unroll
      for (int u = 0; u < 2; ++u) {
        const int n = 4 * t + 2 * u;
        const f32x4 z = {0.f, 0.f, 0.f, 0.f};
        const f32x4 a = mfma16(K[n + 1], qf[2], mfma16(K[n], qf[0], z));
        const f32x4 b = mfma16(K[n + 1], qf[3], mfma16(K[n], qf[1], z));
        xu[u] = (l < 32) ? a : b;
      }
#pragma unroll
      for (int r = 0; r < 4; ++r) {
        const float d = tr32(xu[0][r], xu[1][r]);
        sc[t * 4 + r] = rbf(rbf(d) * 0.08838834764831845f); mx = fmaxf(mx, sc[t * 4 + r]);
      }
    }
    mx = xrow_max(mx);
    float p[8], ls = 0.f;
#pragma unroll
    for (int k = 0; k < 8; ++k) { p[k] = __builtin_amdgcn_exp2f((sc[k] - mx) * 1.4426950408889634f); ls += p[k]; }
    ls = xrow_sum(ls);
    if (l < 2) { lds[L_SM + l * 16 + w] = mx; lds[L_SL + l * 16 + w] = ls; }
    const u32x4 P = {pkbf(p[0], p[1]), pkbf(p[2], p[3]), pkbf(p[4], p[5]), pkbf(p[6], p[7])};
    float o0[8], o1[8];
#pragma unroll
    for (int d = 0; d < 8; ++d) {
      const u32 sel = (d & 1) ? 0x07060302u : 0x05040100u;
      u32x4 B;
#pragma unroll
      for (int k = 0; k < 4; ++k) B[k] = __builtin_amdgcn_perm(V[2 * k + 1][d >> 1], V[2 * k][d >> 1], sel);
      const f32x4 a = mfma16(P, B, f32x4{0.f, 0.f, 0.f, 0.f});
      o0[d] = a[0]; o1[d] = a[1];
    }
    if (l < 16) {
      float* om = &lds[L_OMRG + w * 256 + l * 8];
      *(f32x4*)(om) = f32x4{o0[0], o0[1], o0[2], o0[3]}; *(f32x4*)(om + 4) = f32x4{o0[4], o0[5], o0[6], o0[7]};
      *(f32x4*)(om + 128) = f32x4{o1[0], o1[1], o1[2], o1[3]}; *(f32x4*)(om + 132) = f32x4{o1[4], o1[5], o1[6], o1[7]};
    }
  }
  if (w >= 4 && w < 9) {
    float s;
    for (unsigned spins = 0;; ++spins) {
      bool ok = true; s = 0.f;
#pragma unroll
      for (int k = 0; k < 8; ++k) { const u64 t = gget(gsrc + k * 288); ok &= gok(t, tag); s += gval(t); }
      if (__all(ok) || spins > SPIN_CAP) break;
      __builtin_amdgcn_s_sleep(8);
    }
    const float hb = rbf(rbf(s) + b1f);
    const float r = hb > 0.f ? hb : 0.f;
    const float hv = (hidx < 288) ? rbf(r * r) : 0.f;
    const int hb_ = hidx < 288 ? (hidx / 144) : ((hidx - 288) >> 4), hk = hidx < 288 ? (hidx - hb_ * 144) : (144 + ((hidx - 288) & 15));
    ((u16*)&lds[L_HB])[hb_ * 160 + hk] = (u16)(__float_as_uint(hv) >> 16);
  }
  const int fcol = c * 72 + (h & 7) * 9 + (l < 9 ? l : 0);
  const u16 fx = x[i * D + fcol], fb2 = b2[fcol];
  BARRIER();
  if (w < 5) {
    const int hd = (w < 4) ? (w >> 1) : (g & 1);
    const float mw = lds[L_SM + hd * 16 + cc];
    const float M = row_max(mw);
    const float f = __builtin_amdgcn_exp2f((mw - M) * 1.4426950408889634f);
    if (w < 4) {
      float s0 = 0.f, s1 = 0.f;
#pragma unroll
      for (int k = 0; k < 16; k += 2) {
        s0 = fmaf(__builtin_bit_cast(float, __builtin_amdgcn_readlane(__builtin_bit_cast(int, f), k)), lds[L_OMRG + k * 256 + tid], s0);
        s1 = fmaf(__builtin_bit_cast(float, __builtin_amdgcn_readlane(__builtin_bit_cast(int, f), k + 1)), lds[L_OMRG + (k + 1) * 256 + tid], s1);
      }
      gput(ws + WS_AO + (h * 16 + mi) * 256 + tid, tag, s0 + s1);
    } else {
      const float L = row_sum(f * lds[L_SL + hd * 16 + cc]);
      if (l < 32 && cc < 2) gput(ws + WS_AML + ((h * 2 + hd) * 16 + mi) * MLS + cc, tag, cc ? L : M);
    }
  } else if (w == 5) {
    gu64* pml = ws + WS_AML + ((h * 2 + i) * 16 + ((l & 31) >> 1)) * MLS + (l & 1);
    u64 gml;
    for (unsigned spins = 0;; ++spins) {
      gml = gget(pml);
      if (__all(gok(gml, tag)) || spins > SPIN_CAP) break;
      __builtin_amdgcn_s_sleep(2);
    }
    const float mv = gval(gml);
    const float mM = dpp<0xA0>(mv);
    const float mL = dpp<0xF5>(mv);
    const float Mb = row_max(mM);
    const float wgt = __builtin_amdgcn_exp2f((mM - Mb) * 1.4426950408889634f);
    const float Lb = row_sum((l & 1) ? 0.f : wgt * mL);
    if (l < 32 && !(l & 1)) lds[L_SCL + (l >> 1)] = wgt / Lb;
  } else if (w >= 8 && w < 13) {
    const f32x4 r = gemv_tile<5, 144, 160>((const char*)&lds[L_WST] + 1152 * 16, (const char*)&lds[L_HB], w - 8, l);
    const int col = (w - 8) * 16 + l;
    if (l < 16 && col < 72) {
      gu64* d = ws + WS_FG + (((c * 16 + col / 9) * 2 + 1) * 32 + hq) * 16 + (col % 9);
      gput(d, tag, r[0]); gput(d + 8 * 1024, tag, r[1]);
    }
  }
  BARRIER();
  if (w < 4) {
    const int b = w >> 1;
    gu64* pao = ws + WS_AO + ((h * 16 + b * 8) * 2 + i) * 128 + (tid & 127);
    const f32x4 s0 = *(const f32x4*)&lds[L_SCL + b * 8], s1 = *(const f32x4*)&lds[L_SCL + b * 8 + 4];
    float o;
    for (unsigned spins = 0;; ++spins) {
      bool ok = true; o = 0.f;
#pragma unroll
      for (int k = 0; k < 8; ++k) { const u64 t = gget(pao + k * 256); ok &= gok(t, tag); o = fmaf(gval(t), k < 4 ? s0[k & 3] : s1[k & 3], o); }
      if (__all(ok) || spins > SPIN_CAP) break;
      __builtin_amdgcn_s_sleep(2);
    }
    ((u16*)&lds[L_OB])[tid] = f2bf(o);
  }
  u32x4 wb[4];
  const int colA = w * 16 + cc;
  gu64* dA = ws + WS_FG + (((c * 16 + colA / 9) * 2) * 32 + hq) * 16 + (colA % 9);
  if (w < 5) {
    const char* bp = (const char*)&lds[L_WST] + (8 * g + (cc >> 2)) * 144 + (16 * w + 4 * (cc & 3)) * 2;
#pragma unroll
    for (int ks = 0; ks < 4; ++ks) {
      const u32x2 b0 = trread(bp + ks * 32 * 144), b1_ = trread(bp + ks * 32 * 144 + 4 * 144);
      wb[ks] = u32x4{b0[0], b0[1], b1_[0], b1_[1]};
    }
    asm volatile("" : "+v"(dA));
  }
  const bool fin = (w == 15 && h < 8);
  gu64* pf = ws + WS_FG + ((c * 2 + i) * 8 + h) * 1024 + g * 16 + (cc < 9 ? cc : 0);
  u64 gm[8];
  if (fin) {
#pragma unroll
    for (int k = 0; k < 8; ++k) gm[k] = gget(pf + 512 + k * 64);
  }
  BARRIER();
  if (w < 5) {
    f32x4 r = {0.f, 0.f, 0.f, 0.f};
#pragma unroll
    for (int ks = 0; ks < 4; ++ks) {
      const u32x4 av = *(const u32x4*)((const char*)&lds[L_OB] + (cc & 1) * 256 + (32 * ks + 8 * g) * 2);
      r = mfma16((cc < 2) ? av : u32x4{0u, 0u, 0u, 0u}, wb[ks], r);
    }
    if (l < 16 && colA < 72) { gput(dA, tag, r[0]); gput(dA + 8 * 1024, tag, r[1]); }
  }
  if (fin) {
    float sv, mv = 0.f;
    bool mok = true;
#pragma unroll
    for (int k = 0; k < 8; ++k) { mok &= gok(gm[k], tag); mv += gval(gm[k]); }
    if (!__all(mok)) {
      for (unsigned spins = 0; spins < SPIN_CAP; ++spins) {
        mok = true; mv = 0.f;
#pragma unroll
        for (int k = 0; k < 8; ++k) { const u64 t = gget(pf + 512 + k * 64); mok &= gok(t, tag); mv += gval(t); }
        if (__all(mok)) break;
        __builtin_amdgcn_s_sleep(2);
      }
    }
    __builtin_amdgcn_s_sleep(12);
    for (unsigned spins = 0;; ++spins) {
      bool ok = true; sv = 0.f;
#pragma unroll
      for (int k = 0; k < 8; ++k) { const u64 t = gget(pf + k * 64); ok &= gok(t, tag); sv += gval(t); }
      if (__all(ok) || spins > SPIN_CAP) break;
      __builtin_amdgcn_s_sleep(2);
    }
    sv = xrow_sum(sv); mv = xrow_sum(mv);
    if (l < 9) {
      const float z = rbf(rbf(mv) + bf2f(fb2));
      const float gt = rbf(z / (1.f + expf(-z)));
      const float sg = rbf(rbf(sv) * gt);
      out[i * D + fcol] = f2bf(bf2f(fx) + sg);
    }
    if (bid == 0 && l == 0) __hip_atomic_store(ws + WS_EPOCH, tag, RLX_AGENT);
  }
}

extern "C" void kernel_launch(void* const* d_in, const int* in_sizes, int n_in,
                              void* d_out, int out_size, void* d_ws, size_t ws_size,
                              hipStream_t stream) {
  const u16* x = (const u16*)d_in[0];
  const u16* kc = (const u16*)d_in[1];
  const u16* vc = (const u16*)d_in[2];
  const u16* wq = (const u16*)d_in[3];
  const u16* wo = (const u16*)d_in[4];
  const u16* w1 = (const u16*)d_in[6];
  const u16* b1 = (const u16*)d_in[7];
  const u16* w2 = (const u16*)d_in[8];
  const u16* b2 = (const u16*)d_in[9];
  hipLaunchKernelGGL(fused_decode_kernel, dim3(256), dim3(1024), 0, stream,
                     x, kc, vc, wq, wo, w1, b1, w2, b2, (u16*)d_out, (u64*)d_ws);
}
```
